```python
import torch

def _softmax(x, axis=-1):
    return torch.softmax(x, dim=axis)
def _gelu(x):
    return torch.nn.functional.gelu(x, approximate="tanh")
def _silu(x):
    return torch.nn.functional.silu(x)
def _sigmoid(x):
    return torch.sigmoid(x)
def _layernorm(x, axis=-1, eps=1e-5):
    m = x.mean(dim=axis, keepdim=True)
    v = x.var(dim=axis, keepdim=True, unbiased=False)
    return (x - m) / torch.sqrt(v + eps)
def _scatter_add(tgt, idx, src, axis=0):
    return tgt.clone().index_add_(axis, idx, src)
def _segment_sum(src, seg, n):
    out = torch.zeros((n,) + src.shape[1:], dtype=src.dtype)
    return out.index_add_(0, seg, src)
def _pad(x, pw):
    flat = tuple(v for pair in reversed(pw) for v in pair)
    return torch.nn.functional.pad(x, flat)

kernel_name = 'gated_mix__grouped_kv_mqa__spectral_gfnet__prompt'

def setup_inputs():

    _w = (lambda a: (lambda t: t.to(torch.bfloat16) if t.is_floating_point() else t)(torch.as_tensor(a)))
    def _A_setup():
        B, S, D, H = 1, 2048, 512, 32
        Dh = D // H
        return {
            "x": _w((lambda *s: torch.randn(*s, dtype=torch.bfloat16) * 0.05)(B, S, D)),
            "wq": _w((lambda *s: torch.randn(*s, dtype=torch.bfloat16) * 0.8)(D, H * Dh)),
            "wk": _w((lambda *s: torch.randn(*s, dtype=torch.bfloat16) * 0.8)(D, Dh)),
            "wv": _w((lambda *s: torch.randn(*s, dtype=torch.bfloat16) * 0.05)(D, Dh)),
            "wo": _w((lambda *s: torch.randn(*s, dtype=torch.bfloat16) * 0.05)(H * Dh, D)),
        }
    _A_in = _A_setup()

    def _B_setup():
        B, S, D = 1, 2048, 512
        Df = D // 2 + 1
        return {
            "x": _w((lambda *s: torch.randn(*s, dtype=torch.bfloat16) * 0.05)(B, S, D)),
            "w_r": _w((lambda *s: torch.randn(*s, dtype=torch.bfloat16) * 0.4)(S, Df) * 0.02),
            "w_i": _w((lambda *s: torch.randn(*s, dtype=torch.bfloat16) * 0.8)(S, Df) * 0.02),
        }
    _B_in = _B_setup()

    return {
        "A_x": _A_in["x"],
        "A_wq": _A_in["wq"],
        "A_wk": _A_in["wk"],
        "A_wv": _A_in["wv"],
        "A_wo": _A_in["wo"],
        "B_x": _B_in["x"],
        "B_w_r": _B_in["w_r"],
        "B_w_i": _B_in["w_i"],
        "gate_w": _w((lambda *s: torch.randn(*s, dtype=torch.bfloat16) * 0.05)(512) * 16.0),
    }

def reference(A_x, A_wq, A_wk, A_wv, A_wo, B_x, B_w_r, B_w_i, gate_w):
    def _A_ref(x, wq, wk, wv, wo):
        B, S, D = x.shape
        H = 32
        Dh = D // H
        q = (lambda x, *d: x.permute(*d))((x @ wq).reshape(B, S, H, Dh), 0, 2, 1, 3)
        k = x @ wk
        v = x @ wv
        scores = torch.einsum("bhqd,bkd->bhqk", q, k) * (Dh ** -0.5)
        p = _softmax(scores, axis=-1)
        o = torch.einsum("bhqk,bkd->bhqd", p, v)
        return (lambda x, *d: x.permute(*d))(o, 0, 2, 1, 3).reshape(B, S, H * Dh) @ wo

    def _B_ref(x, w_r, w_i):
        B, S, D = x.shape
        X = (lambda x, axes=(-2,-1): torch.fft.rfft2(x.float(), dim=axes))(x, axes=(-2, -1))
        W = w_r + 1j * w_i
        X = X * W
        y = (lambda x, s, axes=(-2,-1): torch.fft.irfft2(x, s=s, dim=axes).to(torch.bfloat16))(X, (S, D), axes=(-2, -1))
        return y

    x = A_x
    g = _sigmoid((x * gate_w).sum(-1, keepdims=True))
    a = _A_ref(x, A_wq, A_wk, A_wv, A_wo)
    b = _B_ref(x, B_w_r, B_w_i)
    return g * a + (1 - g) * b
```

```cpp
#include <hip/hip_runtime.h>
#include <stdint.h>

#define DI __device__ __forceinline__
#define LAS __attribute__((address_space(3)))
#define GAS __attribute__((address_space(1)))
typedef unsigned short u16;
typedef unsigned long long u64;
typedef short bf16x8 __attribute__((ext_vector_type(8)));
typedef short s16x4 __attribute__((ext_vector_type(4)));
typedef float f32x4 __attribute__((ext_vector_type(4)));
typedef float f32x16 __attribute__((ext_vector_type(16)));
typedef float f2 __attribute__((ext_vector_type(2)));
typedef unsigned u32x4 __attribute__((ext_vector_type(4)));
typedef unsigned u32x2 __attribute__((ext_vector_type(2)));
typedef __bf16 bf16x2_t __attribute__((ext_vector_type(2)));

struct TwTab { float v[4096]; };
constexpr double c_sin_poly(double x) {
  double x2 = x * x, t = x, s = x;
  for (int k = 1; k <= 10; ++k) { t = -t * x2 / ((2 * k) * (2 * k + 1)); s += t; }
  return s;
}
constexpr double c_cos_poly(double x) {
  double x2 = x * x, t = 1.0, s = 1.0;
  for (int k = 1; k <= 10; ++k) { t = -t * x2 / ((2 * k - 1) * (2 * k)); s += t; }
  return s;
}
constexpr TwTab make_tw() {
  TwTab T{};
  const double PI = 3.14159265358979323846264338327950288;
  for (int i = 0; i < 2048; ++i) {
    int quad = i >> 9, r = i & 511;
    double c = 0, s = 0;
    if (r <= 256) { double a = r * (2.0 * PI / 2048.0); c = c_cos_poly(a); s = c_sin_poly(a); }
    else { double a = (512 - r) * (2.0 * PI / 2048.0); c = c_sin_poly(a); s = c_cos_poly(a); }
    double cc = 0, ss = 0;
    if (quad == 0) { cc = c; ss = s; } else if (quad == 1) { cc = -s; ss = c; } else if (quad == 2) { cc = -c; ss = -s; } else { cc = s; ss = -c; }
    T.v[2 * i] = (float)cc; T.v[2 * i + 1] = (float)(-ss);
  }
  return T;
}
__device__ const TwTab TWT = make_tw();
DI f2 tw(int i) { return *(const f2*)&TWT.v[2 * (i & 2047)]; }
struct TwLane { float v[2 * 7 * 64 * 4]; };
constexpr TwLane make_twl() {
  TwLane L{};
  const TwTab T = make_tw();
  for (int f = 0; f < 2; ++f)
    for (int b = 1; b < 8; ++b)
      for (int l = 0; l < 64; ++l) {
        const int ia = (f == 0 ? 4 * l * b : 8 * (l & 31) * b) & 2047, ib = (f == 0 ? 32 * (l & 7) * b : 64 * (l & 3) * b) & 2047;
        float* o = &L.v[((f * 7 + b - 1) * 64 + l) * 4];
        o[0] = T.v[2 * ia]; o[1] = T.v[2 * ia + 1]; o[2] = T.v[2 * ib]; o[3] = T.v[2 * ib + 1];
      }
  return L;
}
__device__ __attribute__((aligned(16))) const TwLane TWL = make_twl();

constexpr unsigned FSTR = 4352;
constexpr unsigned WS_EPOCH = 0;
constexpr unsigned WS_KN  = 1024;
constexpr unsigned WS_SET = FSTR;
constexpr unsigned WS_FT1 = WS_SET + 144 * FSTR;
constexpr unsigned WS_FO  = WS_FT1 + 32 * FSTR;
constexpr unsigned WS_SUM = WS_FO + 32 * FSTR;
constexpr unsigned WS_G   = WS_SUM + 64 * FSTR;
constexpr unsigned WS_KV  = 0x130000;
DI unsigned set_tag_off(int set, int f) { return WS_SET + (set * 16 + (f >> 4)) * FSTR + (f & 15) * 4; }
constexpr unsigned WS_O   = WS_KV + (128u << 10);
constexpr unsigned WS_WS  = WS_O + (2u << 20);
constexpr unsigned WS_A1  = WS_WS + (32u << 10);
constexpr unsigned WS_B1  = WS_A1 + (4u << 20);
constexpr unsigned WS_T1  = WS_B1 + (4u << 20);
constexpr unsigned WS_END = WS_T1 + (4u << 20);
static_assert(WS_G + 8192 <= WS_KV, "ws");

constexpr int KVREC = 2080;
constexpr int L_XA = 0, L_WA = 65536;
constexpr int L_WC = 0, L_XC = 32 * KVREC;
constexpr int L_WKV = 131072;
constexpr int FSCR = 4608;
constexpr int L_YT = 64 * KVREC;
constexpr int L_FS = L_WKV + 16384 - FSCR;
constexpr int L_CTR = L_FS + 4 * FSCR;
constexpr int LDS_BYTES = L_CTR + 256;
static_assert(L_XC + 65536 <= 64 * KVREC && L_YT + 8192 <= L_FS && LDS_BYTES <= 163840, "lds");
enum { C_XA0 = 0, C_XA1, C_XA2, C_XA3, C_GEMM, C_KVOK, C_KV0, C_KV1, C_HALF, C_WO, C_YT, C_XC0, C_XC1, C_XC2, C_XC3, C_A1OK, C_WKV, C_KVM, C_LOOP, C_T1OK, C_KVP, C_ODR, C_TDR, C_H0, C_H1, C_H4, C_H5, C_NCTR };
constexpr int L_TAG = L_CTR + 128;
static_assert(C_NCTR <= 32, "counters");

DI unsigned pk_bf16(float a, float b) { f2 v = {a, b}; return __builtin_bit_cast(unsigned, __builtin_convertvector(v, bf16x2_t)); }
DI float bf_lo(unsigned u) { return __uint_as_float(u << 16); }
DI float bf_hi(unsigned u) { return __uint_as_float(u & 0xffff0000u); }
DI float bf2f(u16 h) { return __uint_as_float((unsigned)h << 16); }
DI f2 cmul(f2 a, f2 b) { return f2{a.x * b.x - a.y * b.y, a.x * b.y + a.y * b.x}; }
DI f2 cconj(f2 a) { return f2{a.x, -a.y}; }
DI f2 mul_pi(f2 a) { return f2{-a.y, a.x}; }
DI f2 mul_mi(f2 a) { return f2{a.y, -a.x}; }

template <bool DEV> DI void glds16(const void* gbase, unsigned voff, unsigned lds_dst) {
  unsigned keep;
  if (DEV) asm volatile("s_mov_b32 %0, m0\n\ts_mov_b32 m0, %3\n\ts_nop 0\n\tglobal_load_lds_dwordx4 %1, %2 sc1\n\ts_mov_b32 m0, %0"
                        : "=&s"(keep) : "v"(voff), "s"(gbase), "s"(lds_dst) : "memory");
  else asm volatile("s_mov_b32 %0, m0\n\ts_mov_b32 m0, %3\n\ts_nop 0\n\tglobal_load_lds_dwordx4 %1, %2\n\ts_mov_b32 m0, %0"
                    : "=&s"(keep) : "v"(voff), "s"(gbase), "s"(lds_dst) : "memory");
}
#define WAIT_VM(n) asm volatile("s_waitcnt vmcnt(" #n ")" ::: "memory")
#define WAIT_LGKM0() asm volatile("s_waitcnt lgkmcnt(0)" ::: "memory")
#define WSYNC() do { asm volatile("" ::: "memory"); __builtin_amdgcn_wave_barrier(); asm volatile("" ::: "memory"); } while (0)

typedef __amdgpu_buffer_rsrc_t rsrc_t;
DI void st16(rsrc_t r, unsigned off, u32x4 v) { __builtin_amdgcn_raw_buffer_store_b128(v, r, off, 0, 16); }
DI void st8(rsrc_t r, unsigned off, u32x2 v) { __builtin_amdgcn_raw_buffer_store_b64(v, r, off, 0, 16); }
DI void st4(rsrc_t r, unsigned off, unsigned v) { __builtin_amdgcn_raw_buffer_store_b32(v, r, off, 0, 16); }
DI u32x4 ld16(rsrc_t r, unsigned off) { return __builtin_amdgcn_raw_buffer_load_b128(r, off, 0, 16); }
DI unsigned ld4(rsrc_t r, unsigned off) { return __builtin_amdgcn_raw_buffer_load_b32(r, off, 0, 16); }
DI f2 as_f2(u32x2 v) { return __builtin_bit_cast(f2, v); }
DI u32x2 as_u2(f2 v) { return __builtin_bit_cast(u32x2, v); }

DI void ctr_arrive(char* lds, int idx, int lane) {
  asm volatile("" ::: "memory");
  if (lane == 0) __hip_atomic_fetch_add((LAS unsigned*)(lds + L_CTR) + idx, 1u, __ATOMIC_RELAXED, __HIP_MEMORY_SCOPE_WORKGROUP);
  asm volatile("" ::: "memory");
}
template <int SLEEP = 1> DI void ctr_wait(char* lds, int idx, unsigned target) {
  while (__builtin_amdgcn_readfirstlane(__hip_atomic_load((LAS unsigned*)(lds + L_CTR) + idx, __ATOMIC_RELAXED, __HIP_MEMORY_SCOPE_WORKGROUP)) < target)
    __builtin_amdgcn_s_sleep(SLEEP);
  asm volatile("" ::: "memory");
}
DI void publish(char* ws, unsigned off, unsigned tag, int lane) {
  WAIT_VM(0);
  if (lane == 0) __hip_atomic_store((GAS unsigned*)(ws + off), tag, __ATOMIC_RELAXED, __HIP_MEMORY_SCOPE_AGENT);
  asm volatile("" ::: "memory");
}
DI void publish4(char* lds, int ctr, rsrc_t rs, unsigned off, unsigned tag, int lane) {
  WAIT_VM(0);
  if (lane == 0) {
    if (__hip_atomic_fetch_add((LAS unsigned*)(lds + L_CTR) + ctr, 1u, __ATOMIC_RELAXED, __HIP_MEMORY_SCOPE_WORKGROUP) == 3) st16(rs, off, u32x4{tag, tag, tag, tag});
  }
  asm volatile("" ::: "memory");
}
template <int SLEEP> DI void wait_tags(rsrc_t rs, unsigned lane_off, unsigned tag) {
  const u64 t0 = __builtin_amdgcn_s_memrealtime();
  for (;;) {
    asm volatile("" ::: "memory");
    const u32x4 v = ld16(rs, lane_off);
    if (__all(v[0] == tag && v[1] == tag && v[2] == tag && v[3] == tag)) break;
    if (__builtin_amdgcn_s_memrealtime() - t0 > 8000000ull) break;
    __builtin_amdgcn_s_sleep(SLEEP);
  }
  asm volatile("" ::: "memory");
}
DI unsigned set_lane_off(int set, int lane) { return WS_SET + (set * 16 + (lane >> 2)) * FSTR + (lane & 3) * 16; }
template <int SLEEP> DI void wait_set(char* ws, rsrc_t rs, int set, bool collector, int replica, unsigned tag, int lane) {
  if (collector) {
    wait_tags<2>(rs, set_lane_off(set, lane), tag);
    if (lane < 8) st16(rs, WS_SUM + ((set - 1) * 8 + lane) * FSTR, u32x4{tag, tag, tag, tag});
    asm volatile("" ::: "memory");
  } else {
    wait_tags<SLEEP>(rs, WS_SUM + ((set - 1) * 8 + replica) * FSTR, tag);
  }
}

template <bool INV> DI void radix8(f2 (&v)[8]) {
  f2 t[8];
  const u64 R = 0x3f3504f33f3504f3ull;
  if (INV) asm(
      "v_pk_add_f32 %8, %0, %4\n\t" "v_pk_add_f32 %12, %0, %4 neg_lo:[0,1] neg_hi:[0,1]\n\t" "v_pk_add_f32 %9, %1, %5\n\t"
      "v_pk_add_f32 %13, %1, %5 neg_lo:[0,1] neg_hi:[0,1]\n\t" "v_pk_add_f32 %10, %2, %6\n\t" "v_pk_add_f32 %14, %2, %6 neg_lo:[0,1] neg_hi:[0,1]\n\t"
      "v_pk_add_f32 %11, %3, %7\n\t" "v_pk_add_f32 %15, %3, %7 neg_lo:[0,1] neg_hi:[0,1]\n\t" "v_pk_add_f32 %5, %13, %13 op_sel:[0,1] op_sel_hi:[1,0] neg_lo:[0,1]\n\t"
      "v_pk_add_f32 %7, %15, %15 op_sel:[0,1] op_sel_hi:[1,0] neg_hi:[0,1]\n\t" "v_pk_add_f32 %0, %8, %10\n\t" "v_pk_add_f32 %1, %8, %10 neg_lo:[0,1] neg_hi:[0,1]\n\t"
      "v_pk_add_f32 %2, %9, %11\n\t" "v_pk_add_f32 %3, %9, %11 neg_lo:[0,1] neg_hi:[0,1]\n\t" "v_pk_add_f32 %8, %12, %14 op_sel:[0,1] op_sel_hi:[1,0] neg_lo:[0,1]\n\t"
      "v_pk_add_f32 %9, %12, %14 op_sel:[0,1] op_sel_hi:[1,0] neg_hi:[0,1]\n\t" "v_pk_add_f32 %10, %5, %7 neg_lo:[0,1] neg_hi:[0,1]\n\t" "v_pk_add_f32 %11, %5, %7\n\t"
      "v_pk_add_f32 %4, %0, %2 neg_lo:[0,1] neg_hi:[0,1]\n\t" "v_pk_add_f32 %0, %0, %2\n\t" "v_pk_add_f32 %6, %1, %3 op_sel:[0,1] op_sel_hi:[1,0] neg_hi:[0,1]\n\t"
      "v_pk_add_f32 %2, %1, %3 op_sel:[0,1] op_sel_hi:[1,0] neg_lo:[0,1]\n\t" "v_pk_fma_f32 %1, %10, %16, %8\n\t"
      "v_pk_fma_f32 %5, %10, %16, %8 neg_lo:[1,0,0] neg_hi:[1,0,0]\n\t" "v_pk_fma_f32 %3, %11, %16, %9 op_sel:[1,0,0] op_sel_hi:[0,1,1] neg_lo:[1,0,0]\n\t"
      "v_pk_fma_f32 %7, %11, %16, %9 op_sel:[1,0,0] op_sel_hi:[0,1,1] neg_hi:[1,0,0]\n\t"
      : "+v"(v[0]), "+v"(v[1]), "+v"(v[2]), "+v"(v[3]), "+v"(v[4]), "+v"(v[5]), "+v"(v[6]), "+v"(v[7]), "=&v"(t[0]), "=&v"(t[1]), "=&v"(t[2]), "=&v"(t[3]), "=&v"(t[4]), "=&v"(t[5]), "=&v"(t[6]), "=&v"(t[7]) : "s"(R));
  else asm(
      "v_pk_add_f32 %8, %0, %4\n\t" "v_pk_add_f32 %12, %0, %4 neg_lo:[0,1] neg_hi:[0,1]\n\t" "v_pk_add_f32 %9, %1, %5\n\t"
      "v_pk_add_f32 %13, %1, %5 neg_lo:[0,1] neg_hi:[0,1]\n\t" "v_pk_add_f32 %10, %2, %6\n\t" "v_pk_add_f32 %14, %2, %6 neg_lo:[0,1] neg_hi:[0,1]\n\t"
      "v_pk_add_f32 %11, %3, %7\n\t" "v_pk_add_f32 %15, %3, %7 neg_lo:[0,1] neg_hi:[0,1]\n\t" "v_pk_add_f32 %5, %13, %13 op_sel:[0,1] op_sel_hi:[1,0] neg_hi:[0,1]\n\t"
      "v_pk_add_f32 %7, %15, %15 op_sel:[0,1] op_sel_hi:[1,0] neg_lo:[0,1]\n\t" "v_pk_add_f32 %0, %8, %10\n\t" "v_pk_add_f32 %1, %8, %10 neg_lo:[0,1] neg_hi:[0,1]\n\t"
      "v_pk_add_f32 %2, %9, %11\n\t" "v_pk_add_f32 %3, %9, %11 neg_lo:[0,1] neg_hi:[0,1]\n\t" "v_pk_add_f32 %8, %12, %14 op_sel:[0,1] op_sel_hi:[1,0] neg_hi:[0,1]\n\t"
      "v_pk_add_f32 %9, %12, %14 op_sel:[0,1] op_sel_hi:[1,0] neg_lo:[0,1]\n\t" "v_pk_add_f32 %10, %5, %7 neg_lo:[0,1] neg_hi:[0,1]\n\t" "v_pk_add_f32 %11, %5, %7\n\t"
      "v_pk_add_f32 %4, %0, %2 neg_lo:[0,1] neg_hi:[0,1]\n\t" "v_pk_add_f32 %0, %0, %2\n\t" "v_pk_add_f32 %6, %1, %3 op_sel:[0,1] op_sel_hi:[1,0] neg_lo:[0,1]\n\t"
      "v_pk_add_f32 %2, %1, %3 op_sel:[0,1] op_sel_hi:[1,0] neg_hi:[0,1]\n\t" "v_pk_fma_f32 %1, %10, %16, %8\n\t"
      "v_pk_fma_f32 %5, %10, %16, %8 neg_lo:[1,0,0] neg_hi:[1,0,0]\n\t" "v_pk_fma_f32 %3, %11, %16, %9 op_sel:[1,0,0] op_sel_hi:[0,1,1] neg_hi:[1,0,0]\n\t"
      "v_pk_fma_f32 %7, %11, %16, %9 op_sel:[1,0,0] op_sel_hi:[0,1,1] neg_lo:[1,0,0]\n\t"
      : "+v"(v[0]), "+v"(v[1]), "+v"(v[2]), "+v"(v[3]), "+v"(v[4]), "+v"(v[5]), "+v"(v[6]), "+v"(v[7]), "=&v"(t[0]), "=&v"(t[1]), "=&v"(t[2]), "=&v"(t[3]), "=&v"(t[4]), "=&v"(t[5]), "=&v"(t[6]), "=&v"(t[7]) : "s"(R));
}
template <bool INV> DI void radix4x2(f2 (&v)[8]) {
  f2 t[8];
  if (INV) asm(
      "v_pk_add_f32 %8, %0, %2\n\t" "v_pk_add_f32 %9, %0, %2 neg_lo:[0,1] neg_hi:[0,1]\n\t" "v_pk_add_f32 %10, %1, %3\n\t"
      "v_pk_add_f32 %11, %1, %3 neg_lo:[0,1] neg_hi:[0,1]\n\t" "v_pk_add_f32 %12, %4, %6\n\t" "v_pk_add_f32 %13, %4, %6 neg_lo:[0,1] neg_hi:[0,1]\n\t"
      "v_pk_add_f32 %14, %5, %7\n\t" "v_pk_add_f32 %15, %5, %7 neg_lo:[0,1] neg_hi:[0,1]\n\t" "v_pk_add_f32 %0, %8, %10\n\t"
      "v_pk_add_f32 %2, %8, %10 neg_lo:[0,1] neg_hi:[0,1]\n\t" "v_pk_add_f32 %1, %9, %11 op_sel:[0,1] op_sel_hi:[1,0] neg_lo:[0,1]\n\t"
      "v_pk_add_f32 %3, %9, %11 op_sel:[0,1] op_sel_hi:[1,0] neg_hi:[0,1]\n\t" "v_pk_add_f32 %4, %12, %14\n\t" "v_pk_add_f32 %6, %12, %14 neg_lo:[0,1] neg_hi:[0,1]\n\t"
      "v_pk_add_f32 %5, %13, %15 op_sel:[0,1] op_sel_hi:[1,0] neg_lo:[0,1]\n\t" "v_pk_add_f32 %7, %13, %15 op_sel:[0,1] op_sel_hi:[1,0] neg_hi:[0,1]\n\t"
      : "+v"(v[0]), "+v"(v[1]), "+v"(v[2]), "+v"(v[3]), "+v"(v[4]), "+v"(v[5]), "+v"(v[6]), "+v"(v[7]), "=&v"(t[0]), "=&v"(t[1]), "=&v"(t[2]), "=&v"(t[3]), "=&v"(t[4]), "=&v"(t[5]), "=&v"(t[6]), "=&v"(t[7]));
  else asm(
      "v_pk_add_f32 %8, %0, %2\n\t" "v_pk_add_f32 %9, %0, %2 neg_lo:[0,1] neg_hi:[0,1]\n\t" "v_pk_add_f32 %10, %1, %3\n\t"
      "v_pk_add_f32 %11, %1, %3 neg_lo:[0,1] neg_hi:[0,1]\n\t" "v_pk_add_f32 %12, %4, %6\n\t" "v_pk_add_f32 %13, %4, %6 neg_lo:[0,1] neg_hi:[0,1]\n\t"
      "v_pk_add_f32 %14, %5, %7\n\t" "v_pk_add_f32 %15, %5, %7 neg_lo:[0,1] neg_hi:[0,1]\n\t" "v_pk_add_f32 %0, %8, %10\n\t"
      "v_pk_add_f32 %2, %8, %10 neg_lo:[0,1] neg_hi:[0,1]\n\t" "v_pk_add_f32 %1, %9, %11 op_sel:[0,1] op_sel_hi:[1,0] neg_hi:[0,1]\n\t"
      "v_pk_add_f32 %3, %9, %11 op_sel:[0,1] op_sel_hi:[1,0] neg_lo:[0,1]\n\t" "v_pk_add_f32 %4, %12, %14\n\t" "v_pk_add_f32 %6, %12, %14 neg_lo:[0,1] neg_hi:[0,1]\n\t"
      "v_pk_add_f32 %5, %13, %15 op_sel:[0,1] op_sel_hi:[1,0] neg_hi:[0,1]\n\t" "v_pk_add_f32 %7, %13, %15 op_sel:[0,1] op_sel_hi:[1,0] neg_lo:[0,1]\n\t"
      : "+v"(v[0]), "+v"(v[1]), "+v"(v[2]), "+v"(v[3]), "+v"(v[4]), "+v"(v[5]), "+v"(v[6]), "+v"(v[7]), "=&v"(t[0]), "=&v"(t[1]), "=&v"(t[2]), "=&v"(t[3]), "=&v"(t[4]), "=&v"(t[5]), "=&v"(t[6]), "=&v"(t[7]));
}
template <bool INV> DI void twiddle7(f2 (&v)[8], const f2 (&w)[7]) {
  f2 t[7];
  if (INV) asm(
      "v_pk_mul_f32 %7, %0, %14 op_sel:[0,0] op_sel_hi:[1,0]\n\t" "v_pk_mul_f32 %8, %1, %15 op_sel:[0,0] op_sel_hi:[1,0]\n\t"
      "v_pk_mul_f32 %9, %2, %16 op_sel:[0,0] op_sel_hi:[1,0]\n\t" "v_pk_mul_f32 %10, %3, %17 op_sel:[0,0] op_sel_hi:[1,0]\n\t"
      "v_pk_mul_f32 %11, %4, %18 op_sel:[0,0] op_sel_hi:[1,0]\n\t" "v_pk_mul_f32 %12, %5, %19 op_sel:[0,0] op_sel_hi:[1,0]\n\t"
      "v_pk_mul_f32 %13, %6, %20 op_sel:[0,0] op_sel_hi:[1,0]\n\t" "v_pk_fma_f32 %0, %0, %14, %7 op_sel:[1,1,0] op_sel_hi:[0,1,1] neg_hi:[1,0,0]\n\t"
      "v_pk_fma_f32 %1, %1, %15, %8 op_sel:[1,1,0] op_sel_hi:[0,1,1] neg_hi:[1,0,0]\n\t" "v_pk_fma_f32 %2, %2, %16, %9 op_sel:[1,1,0] op_sel_hi:[0,1,1] neg_hi:[1,0,0]\n\t"
      "v_pk_fma_f32 %3, %3, %17, %10 op_sel:[1,1,0] op_sel_hi:[0,1,1] neg_hi:[1,0,0]\n\t" "v_pk_fma_f32 %4, %4, %18, %11 op_sel:[1,1,0] op_sel_hi:[0,1,1] neg_hi:[1,0,0]\n\t"
      "v_pk_fma_f32 %5, %5, %19, %12 op_sel:[1,1,0] op_sel_hi:[0,1,1] neg_hi:[1,0,0]\n\t" "v_pk_fma_f32 %6, %6, %20, %13 op_sel:[1,1,0] op_sel_hi:[0,1,1] neg_hi:[1,0,0]\n\t"
      : "+v"(v[1]), "+v"(v[2]), "+v"(v[3]), "+v"(v[4]), "+v"(v[5]), "+v"(v[6]), "+v"(v[7]), "=&v"(t[0]), "=&v"(t[1]), "=&v"(t[2]), "=&v"(t[3]), "=&v"(t[4]), "=&v"(t[5]), "=&v"(t[6]) : "v"(w[0]), "v"(w[1]), "v"(w[2]), "v"(w[3]), "v"(w[4]), "v"(w[5]), "v"(w[6]));
  else asm(
      "v_pk_mul_f32 %7, %0, %14 op_sel:[0,0] op_sel_hi:[1,0]\n\t" "v_pk_mul_f32 %8, %1, %15 op_sel:[0,0] op_sel_hi:[1,0]\n\t"
      "v_pk_mul_f32 %9, %2, %16 op_sel:[0,0] op_sel_hi:[1,0]\n\t" "v_pk_mul_f32 %10, %3, %17 op_sel:[0,0] op_sel_hi:[1,0]\n\t"
      "v_pk_mul_f32 %11, %4, %18 op_sel:[0,0] op_sel_hi:[1,0]\n\t" "v_pk_mul_f32 %12, %5, %19 op_sel:[0,0] op_sel_hi:[1,0]\n\t"
      "v_pk_mul_f32 %13, %6, %20 op_sel:[0,0] op_sel_hi:[1,0]\n\t" "v_pk_fma_f32 %0, %0, %14, %7 op_sel:[1,1,0] op_sel_hi:[0,1,1] neg_lo:[1,0,0]\n\t"
      "v_pk_fma_f32 %1, %1, %15, %8 op_sel:[1,1,0] op_sel_hi:[0,1,1] neg_lo:[1,0,0]\n\t" "v_pk_fma_f32 %2, %2, %16, %9 op_sel:[1,1,0] op_sel_hi:[0,1,1] neg_lo:[1,0,0]\n\t"
      "v_pk_fma_f32 %3, %3, %17, %10 op_sel:[1,1,0] op_sel_hi:[0,1,1] neg_lo:[1,0,0]\n\t" "v_pk_fma_f32 %4, %4, %18, %11 op_sel:[1,1,0] op_sel_hi:[0,1,1] neg_lo:[1,0,0]\n\t"
      "v_pk_fma_f32 %5, %5, %19, %12 op_sel:[1,1,0] op_sel_hi:[0,1,1] neg_lo:[1,0,0]\n\t" "v_pk_fma_f32 %6, %6, %20, %13 op_sel:[1,1,0] op_sel_hi:[0,1,1] neg_lo:[1,0,0]\n\t"
      : "+v"(v[1]), "+v"(v[2]), "+v"(v[3]), "+v"(v[4]), "+v"(v[5]), "+v"(v[6]), "+v"(v[7]), "=&v"(t[0]), "=&v"(t[1]), "=&v"(t[2]), "=&v"(t[3]), "=&v"(t[4]), "=&v"(t[5]), "=&v"(t[6]) : "v"(w[0]), "v"(w[1]), "v"(w[2]), "v"(w[3]), "v"(w[4]), "v"(w[5]), "v"(w[6]));
}
DI void cmul4(f2& a0, f2& a1, f2& a2, f2& a3, f2 w0, f2 w1, f2 w2, f2 w3) {
  f2 t[4];
  asm(
      "v_pk_mul_f32 %4, %0, %8 op_sel:[0,0] op_sel_hi:[1,0]\n\t" "v_pk_mul_f32 %5, %1, %9 op_sel:[0,0] op_sel_hi:[1,0]\n\t"
      "v_pk_mul_f32 %6, %2, %10 op_sel:[0,0] op_sel_hi:[1,0]\n\t" "v_pk_mul_f32 %7, %3, %11 op_sel:[0,0] op_sel_hi:[1,0]\n\t"
      "v_pk_fma_f32 %0, %0, %8, %4 op_sel:[1,1,0] op_sel_hi:[0,1,1] neg_lo:[1,0,0]\n\t" "v_pk_fma_f32 %1, %1, %9, %5 op_sel:[1,1,0] op_sel_hi:[0,1,1] neg_lo:[1,0,0]\n\t"
      "v_pk_fma_f32 %2, %2, %10, %6 op_sel:[1,1,0] op_sel_hi:[0,1,1] neg_lo:[1,0,0]\n\t" "v_pk_fma_f32 %3, %3, %11, %7 op_sel:[1,1,0] op_sel_hi:[0,1,1] neg_lo:[1,0,0]\n\t"
      : "+v"(a0), "+v"(a1), "+v"(a2), "+v"(a3), "=&v"(t[0]), "=&v"(t[1]), "=&v"(t[2]), "=&v"(t[3]) : "v"(w0), "v"(w1), "v"(w2), "v"(w3));
}
struct Tw14 { f2 a[7], b[7]; };
DI void load_tw(Tw14& t, int f, int lane) {
#pragma unroll
  for (int b = 0; b < 7; ++b) { const f32x4 v = *(const f32x4*)&TWL.v[((f * 7 + b) * 64 + lane) * 4]; t.a[b] = f2{v[0], v[1]}; t.b[b] = f2{v[2], v[3]}; }
}
template <bool INV> DI void fft512(f2 (&v)[8], const Tw14& t, char* scr, int lane) {
  f2* sb = (f2*)scr;
  radix8<INV>(v);
  twiddle7<INV>(v, t.a);
#pragma unroll
  for (int b = 0; b < 8; ++b) sb[b * 72 + lane] = v[b];
  WSYNC();
  {
    const int l0 = lane & 7, b = lane >> 3;
#pragma unroll
    for (int l1 = 0; l1 < 8; ++l1) v[l1] = sb[b * 72 + l0 + 8 * l1];
    WSYNC();
    radix8<INV>(v);
    twiddle7<INV>(v, t.b);
#pragma unroll
    for (int c = 0; c < 8; ++c) sb[l0 * 66 + c * 8 + b] = v[c];
  }
  WSYNC();
#pragma unroll
  for (int l0 = 0; l0 < 8; ++l0) v[l0] = sb[l0 * 66 + lane];
  WSYNC();
  radix8<INV>(v);
}
template <bool INV> DI void fft256x2(f2 (&v)[8], const Tw14& t, char* scr, int lane) {
  const int tau = lane >> 5, lam = lane & 31;
  f2* sb = (f2*)scr + tau * 288;
  radix8<INV>(v);
  twiddle7<INV>(v, t.a);
#pragma unroll
  for (int b = 0; b < 8; ++b) sb[b * 36 + lam] = v[b];
  WSYNC();
  {
    const int l0 = lam & 3, b = lam >> 2;
#pragma unroll
    for (int l1 = 0; l1 < 8; ++l1) v[l1] = sb[b * 36 + l0 + 4 * l1];
    WSYNC();
    radix8<INV>(v);
    twiddle7<INV>(v, t.b);
#pragma unroll
    for (int c = 0; c < 8; ++c) sb[l0 * 68 + c * 8 + b] = v[c];
  }
  WSYNC();
  f2 u[8];
#pragma unroll
  for (int ch = 0; ch < 2; ++ch)
#pragma unroll
    for (int l0 = 0; l0 < 4; ++l0) u[ch * 4 + l0] = sb[l0 * 68 + ch * 32 + lam];
  WSYNC();
  radix4x2<INV>(u);
#pragma unroll
  for (int e = 0; e < 4; ++e) { v[2 * e] = u[e]; v[2 * e + 1] = u[4 + e]; }
}

template <bool OTILE> DI void stage_x_piece(const char* base, int tb, int kc, int r4, unsigned lds_dst, int lane) {
  const int grow = OTILE ? (64 * tb + 4 * r4) : (8 * tb + (r4 >> 1) + 1024 * (r4 & 1));
  const unsigned voff = (lane >> 4) * (OTILE ? 1024 : 262144) + (((lane ^ (4 * r4 + (lane >> 4))) & 15) << 4);
  glds16<OTILE>(base + (size_t)grow * 1024 + kc * 256, voff, lds_dst + kc * 16384 + r4 * 1024);
}
DI void stage_w_piece(const u16* w, int cb, int i, unsigned lds_dst, int lane) {
  glds16<false>((const char*)w + i * 8192 + cb * 128, (lane >> 3) * 1024 + (((lane & 7) ^ ((lane >> 2) & 4)) << 4), lds_dst + i * 1024);
}
DI bf16x8 tr_frag(const char* p0, const char* p1) {
  s16x4 lo = __builtin_amdgcn_ds_read_tr16_b64_v4i16((LAS s16x4*)p0);
  s16x4 hi = __builtin_amdgcn_ds_read_tr16_b64_v4i16((LAS s16x4*)p1);
  return __builtin_shufflevector(lo, hi, 0, 1, 2, 3, 4, 5, 6, 7);
}
DI void gemm_chunk(const char* xt, const char* wt, int kc, int th, int ch, int lane, f32x16& acc) {
  const int h2 = lane >> 5, gi = (lane >> 4) & 1, q = (lane >> 2) & 3, p = lane & 3, tok = lane & 31;
  const int P = ((p & 1) << 1) | (p >> 1);
  const char* wa = wt + (8 * h2 + q) * 128 + (((4 * ch + 2 * gi + (P >> 1)) ^ ((q >> 1) << 2)) << 4) + 8 * (P & 1) + kc * 16384;
  const char* xa = xt + (32 * th + tok) * 256 + kc * 16384;
  bf16x8 a[8], b[8];
#pragma unroll
  for (int s = 0; s < 8; ++s) {
    a[s] = tr_frag(wa + s * 2048, wa + s * 2048 + 512);
    b[s] = *(const bf16x8*)(xa + (((2 * s + h2) ^ (tok & 15)) << 4));
  }
  __builtin_amdgcn_sched_barrier(0);
#pragma unroll
  for (int s = 0; s < 8; ++s) acc = __builtin_amdgcn_mfma_f32_32x32x16_bf16(a[s], b[s], acc, 0, 0, 0);
  asm volatile("" : "+v"(acc));
}

DI void gemm_load_w(const char* wt, int ch, int lane, bf16x8 (&a)[32]) {
  const int h2 = lane >> 5, gi = (lane >> 4) & 1, q = (lane >> 2) & 3, p = lane & 3;
  const int P = ((p & 1) << 1) | (p >> 1);
  const char* wa = wt + (8 * h2 + q) * 128 + (((4 * ch + 2 * gi + (P >> 1)) ^ ((q >> 1) << 2)) << 4) + 8 * (P & 1);
#pragma unroll
  for (int s = 0; s < 32; ++s) a[s] = tr_frag(wa + s * 2048, wa + s * 2048 + 512);
}
DI void gemm_load_x(const char* xt, int kc, int th, int lane, bf16x8 (&b)[8]) {
  const int h2 = lane >> 5, tok = lane & 31;
  const char* xa = xt + (32 * th + tok) * 256 + kc * 16384;
#pragma unroll
  for (int s = 0; s < 8; ++s) b[s] = *(const bf16x8*)(xa + (((2 * s + h2) ^ (tok & 15)) << 4));
  __builtin_amdgcn_sched_barrier(0);
}
template <int KC> DI void gemm_mm(const bf16x8 (&a)[32], const bf16x8 (&b)[8], f32x16& acc) {
#pragma unroll
  for (int s = 0; s < 8; ++s) acc = __builtin_amdgcn_mfma_f32_32x32x16_bf16(a[8 * KC + s], b[s], acc, 0, 0, 0);
  asm volatile("" : "+v"(acc));
}
#define DPP(v, ctrl) __builtin_amdgcn_update_dpp(0, (int)(v), ctrl, 0xF, 0xF, true)
DI float sum_rows(float v) {
  const unsigned u = __float_as_uint(v);
  const auto a = __builtin_amdgcn_permlane16_swap(u, u, false, false);
  const unsigned w = __float_as_uint(__uint_as_float(a[0]) + __uint_as_float(a[1]));
  const auto b = __builtin_amdgcn_permlane32_swap(w, w, false, false);
  return __uint_as_float(b[0]) + __uint_as_float(b[1]);
}
DI unsigned wave_umax(unsigned v) {
  v = max(v, (unsigned)DPP(v, 0xB1)); v = max(v, (unsigned)DPP(v, 0x4E)); v = max(v, (unsigned)DPP(v, 0x141)); v = max(v, (unsigned)DPP(v, 0x140));
  const auto a = __builtin_amdgcn_permlane16_swap(v, v, false, false);
  v = max(a[0], a[1]);
  const auto b = __builtin_amdgcn_permlane32_swap(v, v, false, false);
  return max(b[0], b[1]);
}
DI float wave_sum(float v) {
  v += __int_as_float(DPP(__float_as_int(v), 0xB1)); v += __int_as_float(DPP(__float_as_int(v), 0x4E));
  v += __int_as_float(DPP(__float_as_int(v), 0x141)); v += __int_as_float(DPP(__float_as_int(v), 0x140));
  return sum_rows(v);
}
DI void kv_piece(const u16* x, const u16* w, rsrc_t rs, char* lds, unsigned lb, int tb, int cb, int wave, int lane) {
  const int tg = cb >> 1, g = lane >> 4, q = (lane >> 2) & 3, pp = lane & 3, tok = lane & 15;
#pragma unroll
  for (int i = 0; i < 4; ++i) glds16<false>((const char*)w + (4 * wave + i) * 1024, (lane ^ ((lane >> 1) & 8)) * 16, lb + L_WKV + (4 * wave + i) * 1024);
  const int t = 16 * tg + tok;
  const u16* xr = x + (size_t)(8 * tb + (t >> 3) + 256 * (t & 7)) * 512 + 8 * g + 128 * wave;
  bf16x8 xf[4], wf[4];
#pragma unroll
  for (int s = 0; s < 4; ++s) xf[s] = *(const bf16x8*)(xr + 32 * s);
  __builtin_amdgcn_s_barrier();
  WAIT_VM(0);
  const char* wa0 = lds + L_WKV + wave * 4096 + (((8 * g + q) ^ (4 * (g & 1))) * 32) + 8 * pp;
  const char* wa1 = lds + L_WKV + wave * 4096 + (((8 * g + q + 4) ^ (4 * (g & 1))) * 32) + 8 * pp;
#pragma unroll
  for (int s = 0; s < 4; ++s) wf[s] = tr_frag(wa0 + s * 1024, wa1 + s * 1024);
  f32x4 acc0 = {0, 0, 0, 0};
#pragma unroll
  for (int s = 0; s < 4; ++s) acc0 = __builtin_amdgcn_mfma_f32_16x16x32_bf16((cb & 1) ? xf[s] : wf[s], (cb & 1) ? wf[s] : xf[s], acc0, 0, 0, 0);
  if (wave != 3) {
    *(f32x4*)(lds + L_WKV + wave * 4096 + lane * 16) = acc0;
    WAIT_LGKM0();
    ctr_arrive(lds, C_KVP, lane);
    return;
  }
  ctr_wait(lds, C_KVP, 3);
#pragma unroll
  for (int i = 0; i < 3; ++i) acc0 += *(const f32x4*)(lds + L_WKV + i * 4096 + lane * 16);
  const u32x2 pkd = {pk_bf16(acc0[0], acc0[1]), pk_bf16(acc0[2], acc0[3])};
  const unsigned rec = WS_KV + (2 * tb + (tg >> 1)) * 2048;
  if (cb & 1) {
    const int d = tok, rho = (d & 3) + 4 * (d >> 3) + 8 * ((d >> 2) & 1);
    st8(rs, rec + 1024 + (tg & 1) * 512 + (g & 1) * 256 + rho * 16 + (g >> 1) * 8, pkd);
  } else {
    st8(rs, rec + (g >> 1) * 512 + (16 * (tg & 1) + tok) * 16 + (g & 1) * 8, pkd);
    const float a0 = bf_lo(pkd[0]), a1 = bf_hi(pkd[0]), a2 = bf_lo(pkd[1]), a3 = bf_hi(pkd[1]);
    float n2 = a0 * a0 + a1 * a1 + a2 * a2 + a3 * a3;
    n2 = sum_rows(n2);
    const unsigned mx = wave_umax(__float_as_uint(n2));
    if (lane == 0) st4(rs, WS_KN + (tb * 4 + tg) * 4, mx);
  }
  WAIT_LGKM0();
}

DI void gate_stage(const u16* gw, rsrc_t rs, int n0, int p, u32x4 g0, u32x4 g1, int lane) {
  const u32x4 gv = *(const u32x4*)(gw + lane * 8);
  float s0 = 0.f, s1 = 0.f;
#pragma unroll
  for (int i = 0; i < 4; ++i) {
    const unsigned a0 = g0[i], a1 = g1[i], gq = gv[i];
    s0 = __builtin_amdgcn_fdot2_f32_bf16(__builtin_bit_cast(bf16x2_t, a0), __builtin_bit_cast(bf16x2_t, gq), s0, false);
    s1 = __builtin_amdgcn_fdot2_f32_bf16(__builtin_bit_cast(bf16x2_t, a1), __builtin_bit_cast(bf16x2_t, gq), s1, false);
  }
  s0 = wave_sum(s0); s1 = wave_sum(s1);
  const float gg = __builtin_amdgcn_rcpf(1.f + __expf(-((lane & 1) ? s1 : s0)));
  if (lane < 2) st4(rs, WS_G + ((n0 >> 3) * 64 + (n0 & 7) * 8 + 2 * p + lane) * 4, __float_as_uint(gg));
}
struct __attribute__((packed, aligned(2))) U4H { u16 v[4]; };
DI int w_row(int c, int i, int lane) { const int rho = 192 * (c - 1) + 64 * i + lane; return rho < 512 ? rho : 511; }
DI void w_load(const u16* wr, const u16* wi, int cq, int pf, int c, int lane, U4H (&re)[3], U4H (&im)[3]) {
#pragma unroll
  for (int i = 0; i < 3; ++i) {
    const int rho = w_row(c, i, lane), j = (rho >> 8) == 0 ? pf : (pf == 0 ? 4 : 8 - pf);
    const int e = (8 * (rho & 255) + j) * 257 + 4 * cq;
    re[i] = *(const U4H*)(wr + e); im[i] = *(const U4H*)(wi + e);
  }
}
DI void kv_dma(const char* ws, unsigned lb, int piece, int lane) {
  glds16<true>(ws + WS_KV + piece * 1024, lane * 16, lb + (piece >> 1) * KVREC + (piece & 1) * 1024);
}
DI void kv_rest(const char* ws, char* lds, unsigned lb, int p, int lane, bool block, int& state) {
  if (state != 0) return;
  if (block) ctr_wait<2>(lds, C_KVM, 4);
  else if (__builtin_amdgcn_readfirstlane(__hip_atomic_load((LAS unsigned*)(lds + L_CTR) + C_KVM, __ATOMIC_RELAXED, __HIP_MEMORY_SCOPE_WORKGROUP)) < 4) return;
#pragma unroll 1
  for (int i = 0; i < 24; ++i) kv_dma(ws, lb, 32 + 4 * i + p, lane);
  state = 1;
}
DI void f1_stage(const u16* x, const u16* gw, const char* ws, unsigned lb, int& kvs, Tw14& t2, rsrc_t rs, char* lds, int n0, int p, const Tw14& t5, char* scr, int lane) {
  u32x4 xr[8];
#pragma unroll
  for (int m = 0; m < 8; ++m) xr[m] = *(const u32x4*)(x + (size_t)(n0 + 256 * m) * 512 + lane * 8);
  gate_stage(gw, rs, n0, p, p == 0 ? xr[0] : p == 1 ? xr[2] : p == 2 ? xr[4] : xr[6], p == 0 ? xr[1] : p == 1 ? xr[3] : p == 2 ? xr[5] : xr[7], lane);
  f2 c[8];
#pragma unroll
  for (int i = 0; i < 8; ++i) c[i] = f2{0.f, 0.f};
#pragma unroll
  for (int m = 0; m < 8; ++m) {
    const f2 cf = p == 0 ? f2{1.f, (m & 1) ? -1.f : 1.f} : tw(256 * m * p);
#pragma unroll
    for (int i = 0; i < 4; ++i) { c[2 * i] += cf * bf_lo(xr[m][i]); c[2 * i + 1] += cf * bf_hi(xr[m][i]); }
  }
  f2* sb = (f2*)scr;
  kv_rest(ws, lds, lb, p, lane, false, kvs);
  if (p == 0) ctr_wait(lds, C_WKV, 1);
#pragma unroll
  for (int i = 0; i < 8; ++i) sb[9 * lane + i] = c[i];
  WSYNC();
#pragma unroll
  for (int a = 0; a < 8; ++a) c[a] = sb[lane + (lane >> 3) + 72 * a];
  WSYNC();
  kv_rest(ws, lds, lb, p, lane, false, kvs);
  fft512<false>(c, t5, scr, lane);
  kv_rest(ws, lds, lb, p, lane, false, kvs);
  load_tw(t2, 1, lane);
#pragma unroll
  for (int a = 4; a < 8; ++a) sb[lane + 64 * (a - 4)] = c[a];
  WSYNC();
  f2 al1, be1, al2, be2;
  if (p == 0) { const f2 t4 = tw(4 * n0); al1 = f2{0.5f, 0.f}; be1 = al1; al2 = mul_mi(t4) * 0.5f; be2 = mul_pi(t4) * 0.5f; }
  else { al1 = tw(n0 * p); be1 = f2{0.f, 0.f}; al2 = be1; be2 = tw(n0 * (8 - p)); }
  u32x4 ov[4];
#pragma unroll
  for (int cc = 0; cc < 4; ++cc) {
    const int k2 = lane + 64 * cc;
    const f2 a = c[cc];
    const f2 pm = sb[k2 == 0 ? 0 : 256 - k2];
    f2 o1, o2;
    if (k2 == 0) {
      const f2 g1a = cmul(al1, a) + cmul(be1, cconj(a)), g1b = cmul(al1, pm) + cmul(be1, cconj(pm));
      const f2 g2a = cmul(al2, a) + cmul(be2, cconj(a)), g2b = cmul(al2, pm) + cmul(be2, cconj(pm));
      o1 = g1a + mul_pi(g1b); o2 = g2a + mul_pi(g2b);
    } else {
      o1 = cmul(al1, a) + cmul(be1, cconj(pm)); o2 = cmul(al2, a) + cmul(be2, cconj(pm));
    }
    ov[cc] = __builtin_bit_cast(u32x4, f32x4{o1.x, o1.y, o2.x, o2.y});
  }
  if (kvs == 1) { WAIT_VM(0); ctr_arrive(lds, C_KV1, lane); kvs = 2; }
#pragma unroll
  for (int cc = 0; cc < 4; ++cc) st16(rs, WS_A1 + n0 * 16384 + p * 4096 + (lane + 64 * cc) * 16, ov[cc]);
  WSYNC();
}
DI void wspec_load(const u16* wr, const u16* wi, int b, int p, int lane, u16 (&v)[8]) {
  const int j = (lane & 1) == 0 ? p : (p == 0 ? 4 : 8 - p), k1 = 8 * b + j, k1m = (2048 - k1) & 2047;
  v[0] = wr[k1 * 257]; v[1] = wi[k1 * 257]; v[2] = wr[k1m * 257]; v[3] = wi[k1m * 257];
  v[4] = wr[k1 * 257 + 256]; v[5] = wi[k1 * 257 + 256]; v[6] = wr[k1m * 257 + 256]; v[7] = wi[k1m * 257 + 256];
}
DI void wspec_store(const u16 (&v)[8], rsrc_t rs, int b, int p, int lane) {
  const int j = (lane & 1) == 0 ? p : (p == 0 ? 4 : 8 - p);
  const f2 w0s = f2{bf2f(v[0]) + bf2f(v[2]), bf2f(v[1]) - bf2f(v[3])} * 0.5f, wns = f2{bf2f(v[4]) + bf2f(v[6]), bf2f(v[5]) - bf2f(v[7])} * 0.5f;
  const f2 wp = (w0s + wns) * 0.5f, wm = (w0s - wns) * 0.5f;
  if (lane < 2) st16(rs, WS_WS + (j * 256 + b) * 16, __builtin_bit_cast(u32x4, f32x4{wp.x, wp.y, wm.x, wm.y}));
}

DI void f2_stage(const U4H (&re)[3], const U4H (&im)[3], rsrc_t rs, char* lds, int cq, int pf, int c, const Tw14& t2, int lane) {
  const int tau = lane >> 5, lam = lane & 31;
  const int j = tau == 0 ? pf : (pf == 0 ? 4 : 8 - pf);
  char* scr = lds + L_FS + c * FSCR;
  {
    u32x4 d[4];
#pragma unroll
    for (int i = 0; i < 4; ++i) d[i] = ld16(rs, WS_A1 + (64 * c + 16 * i + (lane >> 2)) * 16384 + pf * 4096 + cq * 64 + (lane & 3) * 16);
    ctr_wait(lds, C_H0, 4);
    if (c != 0) {
#pragma unroll
      for (int i = 0; i < 3; ++i)
#pragma unroll
        for (int q = 0; q < 4; ++q)
          *(unsigned*)(lds + L_YT + q * 2048 + w_row(c, i, lane) * 4) = (unsigned)re[i].v[q] | ((unsigned)im[i].v[q] << 16);
    }
#pragma unroll
    for (int i = 0; i < 4; ++i) *(u32x4*)(lds + L_FS + (lane & 3) * (FSCR + 32) + (64 * c + 16 * i + (lane >> 2)) * 16) = d[i];
  }
  WAIT_LGKM0();
  ctr_arrive(lds, C_H1, lane); ctr_wait(lds, C_H1, 4);
  f2 v[8];
  unsigned w[8];
#pragma unroll
  for (int a = 0; a < 8; ++a) v[a] = *(const f2*)(scr + c * 32 + tau * 8 + (lam + 32 * a) * 16);
#pragma unroll
  for (int a = 0; a < 8; ++a) w[a] = *(const unsigned*)(lds + L_YT + c * 2048 + (tau * 256 + lam + 32 * a) * 4);
  WSYNC();
  fft256x2<false>(v, t2, scr, lane);
  if (cq != 0 || c != 0) {
    cmul4(v[0], v[1], v[2], v[3], f2{bf_lo(w[0]), bf_hi(w[0])}, f2{bf_lo(w[1]), bf_hi(w[1])}, f2{bf_lo(w[2]), bf_hi(w[2])}, f2{bf_lo(w[3]), bf_hi(w[3])});
    cmul4(v[4], v[5], v[6], v[7], f2{bf_lo(w[4]), bf_hi(w[4])}, f2{bf_lo(w[5]), bf_hi(w[5])}, f2{bf_lo(w[6]), bf_hi(w[6])}, f2{bf_lo(w[7]), bf_hi(w[7])});
  } else {
    f2* sb = (f2*)scr;
#pragma unroll
    for (int a = 0; a < 8; ++a) sb[tau * 256 + lam + 32 * a] = v[a];
    WSYNC();
#pragma unroll
    for (int a = 0; a < 8; ++a) {
      const int k1 = 8 * (lam + 32 * a) + j, k1m = (2048 - k1) & 2047;
      const f2 cm = cconj(sb[(pf == 0 ? tau : 1 - tau) * 256 + (k1m >> 3)]);
      const u32x4 wq4 = ld16(rs, WS_WS + (j * 256 + lam + 32 * a) * 16);
      const f32x4 wf = __builtin_bit_cast(f32x4, wq4);
      v[a] = cmul(v[a], f2{wf[0], wf[1]}) + cmul(cm, f2{wf[2], wf[3]});
    }
    WSYNC();
  }
  fft256x2<true>(v, t2, scr, lane);
#pragma unroll
  for (int a = 0; a < 8; ++a) *(f2*)(scr + c * 32 + tau * 8 + (lam + 32 * a) * 16) = v[a];
  WAIT_LGKM0();
  ctr_arrive(lds, C_H4, lane); ctr_wait(lds, C_H4, 4);
  u32x4 o[4];
#pragma unroll
  for (int i = 0; i < 4; ++i) o[i] = *(const u32x4*)(lds + L_FS + (lane & 3) * (FSCR + 32) + (64 * c + 16 * i + (lane >> 2)) * 16);
#pragma unroll
  for (int i = 0; i < 4; ++i) st16(rs, WS_B1 + pf * (1 << 20) + cq * 16384 + (64 * c + 16 * i) * 64 + lane * 16, o[i]);
  WAIT_LGKM0();
  ctr_arrive(lds, C_H5, lane);
}

DI void f3_stage(rsrc_t rs, int n0, int p, const Tw14& t5, char* scr, int lane) {
  const float SC = 1.0f / (2048.0f * 512.0f);
  const int jB = p == 0 ? 4 : 8 - p;
  u32x4 d[4];
#pragma unroll
  for (int cc = 0; cc < 4; ++cc) d[cc] = ld16(rs, WS_B1 + p * (1 << 20) + (16 * cc + (lane >> 2)) * 16384 + n0 * 64 + (lane & 3) * 16);
  const f2 tA = cconj(tw(n0 * p)) * SC, tB = cconj(tw(n0 * jB)) * SC;
  f2 v[8], da[4], db[4];
  f2* sb = (f2*)scr;
#pragma unroll
  for (int cc = 0; cc < 4; ++cc) { da[cc] = as_f2(u32x2{d[cc][0], d[cc][1]}); db[cc] = as_f2(u32x2{d[cc][2], d[cc][3]}); }
  cmul4(da[0], da[1], da[2], da[3], tA, tA, tA, tA);
  cmul4(db[0], db[1], db[2], db[3], tB, tB, tB, tB);
  f2 g1[4];
  if (p == 0) {
#pragma unroll
    for (int cc = 0; cc < 4; ++cc) { const f2 a = da[cc], b = db[cc]; v[cc] = f2{a.x - b.y, a.y + b.x}; g1[cc] = f2{a.x + b.y, b.x - a.y}; }
    if (lane == 0) { v[0] = f2{da[0].x, db[0].x}; g1[0] = f2{da[0].y, db[0].y}; }
  } else {
#pragma unroll
    for (int cc = 0; cc < 4; ++cc) { v[cc] = da[cc]; g1[cc] = cconj(db[cc]); }
    if (lane == 0) { const f2 a = da[0], bc = g1[0]; v[0] = (a + bc) * 0.5f; g1[0] = mul_mi(a - bc) * 0.5f; }
  }
  sb[(256 - lane) & 255] = g1[0];
#pragma unroll
  for (int cc = 1; cc < 4; ++cc) sb[256 - lane - 64 * cc] = g1[cc];
  WSYNC();
#pragma unroll
  for (int a = 4; a < 8; ++a) v[a] = sb[lane + 64 * (a - 4)];
  WSYNC();
  fft512<true>(v, t5, scr, lane);
#pragma unroll
  for (int e = 0; e < 8; ++e) st8(rs, WS_T1 + (n0 * 4 + p) * 4096 + (lane + 64 * e) * 8, as_u2(v[e]));
}
struct YIn { u32x4 t[4], g0, g1; };
DI void y_load(rsrc_t rs, int tb, int cb, int p, int lane, YIn& in) {
  const int a = 2 * p + (lane >> 5), c2 = lane & 31;
#pragma unroll
  for (int i = 0; i < 4; ++i) in.t[i] = ld16(rs, WS_T1 + ((8 * tb + a) * 4 + i) * 4096 + (64 * cb + 2 * c2) * 8);
  in.g0 = ld16(rs, WS_G + (64 * tb + 8 * a) * 4); in.g1 = ld16(rs, WS_G + (64 * tb + 8 * a + 4) * 4);
  WAIT_VM(0);
}
DI void y_stage(const YIn& in, char* lds, int p, int lane) {
  const int a = 2 * p + (lane >> 5), c2 = lane & 31;
  const u32x4 (&t)[4] = in.t;
  const u32x4 g0 = in.g0, g1 = in.g1;
  float y[2][8];
#pragma unroll
  for (int e = 0; e < 2; ++e) {
    const float R2 = 1.41421356237309504880f;
    const float t0 = __uint_as_float(t[0][2 * e]), t4 = __uint_as_float(t[0][2 * e + 1]);
    const float r1 = __uint_as_float(t[1][2 * e]), i1 = __uint_as_float(t[1][2 * e + 1]);
    const float r2 = 2.f * __uint_as_float(t[2][2 * e]), i2 = 2.f * __uint_as_float(t[2][2 * e + 1]);
    const float r3 = __uint_as_float(t[3][2 * e]), i3 = __uint_as_float(t[3][2 * e + 1]);
    const float e0 = t0 + t4, e1 = t0 - t4, sa = 2.f * (r1 + r3), sb = 2.f * (i1 - i3), u = R2 * (r1 - r3), w = R2 * (i1 + i3);
    y[e][0] = e0 + r2 + sa; y[e][4] = e0 + r2 - sa;
    y[e][2] = e0 - r2 - sb; y[e][6] = e0 - r2 + sb;
    y[e][1] = e1 - i2 + (u - w); y[e][5] = e1 - i2 - (u - w);
    y[e][3] = e1 + i2 - (u + w); y[e][7] = e1 + i2 + (u + w);
  }
#pragma unroll
  for (int m = 0; m < 8; ++m) {
    const float og = 1.f - __uint_as_float(m < 4 ? g0[m & 3] : g1[m & 3]);
    *(unsigned*)(lds + L_YT + (8 * a + m) * 128 + (((c2 >> 2) ^ m) << 4) + (c2 & 3) * 4) = pk_bf16(og * y[0][m], og * y[1][m]);
  }
  WAIT_LGKM0();
}
#define ATTN_ASM \
  "v_mov_b32 v160, %[ca]\n\t" "v_mov_b32 v176, %[cb]\n\t" "v_mov_b32 v161, %[ca]\n\t" "v_mov_b32 v177, %[cb]\n\t" "v_mov_b32 v162, %[ca]\n\t" "v_mov_b32 v178, %[cb]\n\t" \
  "v_mov_b32 v163, %[ca]\n\t" "v_mov_b32 v179, %[cb]\n\t" "v_mov_b32 v164, %[ca]\n\t" "v_mov_b32 v180, %[cb]\n\t" "v_mov_b32 v165, %[ca]\n\t" "v_mov_b32 v181, %[cb]\n\t" \
  "v_mov_b32 v166, %[ca]\n\t" "v_mov_b32 v182, %[cb]\n\t" "v_mov_b32 v167, %[ca]\n\t" "v_mov_b32 v183, %[cb]\n\t" "v_mov_b32 v168, %[ca]\n\t" "v_mov_b32 v184, %[cb]\n\t" \
  "v_mov_b32 v169, %[ca]\n\t" "v_mov_b32 v185, %[cb]\n\t" "v_mov_b32 v170, %[ca]\n\t" "v_mov_b32 v186, %[cb]\n\t" "v_mov_b32 v171, %[ca]\n\t" "v_mov_b32 v187, %[cb]\n\t" \
  "v_mov_b32 v172, %[ca]\n\t" "v_mov_b32 v188, %[cb]\n\t" "v_mov_b32 v173, %[ca]\n\t" "v_mov_b32 v189, %[cb]\n\t" "v_mov_b32 v174, %[ca]\n\t" "v_mov_b32 v190, %[cb]\n\t" \
  "v_mov_b32 v175, %[ca]\n\t" "v_mov_b32 v191, %[cb]\n\t" "v_mov_b32 v128, 0\n\t" "v_mov_b32 v144, 0\n\t" "v_mov_b32 v129, 0\n\t" "v_mov_b32 v145, 0\n\t" \
  "v_mov_b32 v130, 0\n\t" "v_mov_b32 v146, 0\n\t" "v_mov_b32 v131, 0\n\t" "v_mov_b32 v147, 0\n\t" "v_mov_b32 v132, 0\n\t" "v_mov_b32 v148, 0\n\t" "v_mov_b32 v133, 0\n\t" \
  "v_mov_b32 v149, 0\n\t" "v_mov_b32 v134, 0\n\t" "v_mov_b32 v150, 0\n\t" "v_mov_b32 v135, 0\n\t" "v_mov_b32 v151, 0\n\t" "v_mov_b32 v136, 0\n\t" "v_mov_b32 v152, 0\n\t" \
  "v_mov_b32 v137, 0\n\t" "v_mov_b32 v153, 0\n\t" "v_mov_b32 v138, 0\n\t" "v_mov_b32 v154, 0\n\t" "v_mov_b32 v139, 0\n\t" "v_mov_b32 v155, 0\n\t" "v_mov_b32 v140, 0\n\t" \
  "v_mov_b32 v156, 0\n\t" "v_mov_b32 v141, 0\n\t" "v_mov_b32 v157, 0\n\t" "v_mov_b32 v142, 0\n\t" "v_mov_b32 v158, 0\n\t" "v_mov_b32 v143, 0\n\t" "v_mov_b32 v159, 0\n\t" \
  "v_mov_b32 v204, 0\n\t" "v_mov_b32 v228, 0\n\t" "v_mov_b32 v205, 0\n\t" "v_mov_b32 v229, 0\n\t" "v_mov_b32 v206, 0\n\t" "v_mov_b32 v230, 0\n\t" "v_mov_b32 v207, 0\n\t" \
  "v_mov_b32 v231, 0\n\t" "s_mov_b32 %[cnt], 16\n\t" "v_mov_b32 v241, 1\n\t" "s_waitcnt vmcnt(0)\n\t" "s_mov_b64 exec, 1\n\t" \
  "ds_add_u32 %[ctr], %[inc] offset:%c[o0]\n\t" "s_mov_b64 exec, -1\n\t" "5:\n\t" "ds_read_b32 v240, %[ctr] offset:%c[o0]\n\t" "s_waitcnt lgkmcnt(0)\n\t" \
  "v_readfirstlane_b32 %[ev], v240\n\t" "s_nop 1\n\t" "s_cmp_lt_u32 %[ev], 4\n\t" "s_cbranch_scc1 5b\n\t" "ds_read_b128 v[208:211], %[ka]\n\t" \
  "ds_read_b128 v[216:219], %[va0]\n\t" "ds_read_b128 v[224:227], %[va1]\n\t" "s_mov_b32 %[ev], 13\n\t" "s_setprio 0\n\t" "s_waitcnt lgkmcnt(2)\n\t" \
  "v_mfma_f32_32x32x16_bf16 v[96:111], v[208:211], v[232:235], v[160:175]\n\t" "s_waitcnt lgkmcnt(0)\n\t" "s_nop 7\n\t" "s_nop 2\n\t" "1:\n\t" "v_exp_f32 v96, v96\n\t" \
  "v_exp_f32 v97, v97\n\t" "v_mfma_f32_32x32x16_bf16 v[144:159], v[228:231], v[204:207], v[144:159]\n\t" "ds_read_b128 v[212:215], %[ka] offset:2080\n\t" \
  "ds_read_b128 v[220:223], %[va0] offset:2080\n\t" "ds_read_b128 v[228:231], %[va1] offset:2080\n\t" "v_exp_f32 v98, v98\n\t" "v_exp_f32 v99, v99\n\t" \
  "v_exp_f32 v100, v100\n\t" "v_exp_f32 v101, v101\n\t" "v_exp_f32 v102, v102\n\t" "v_mfma_f32_32x32x16_bf16 v[112:127], v[208:211], v[236:239], v[176:191]\n\t" \
  "v_exp_f32 v103, v103\n\t" "v_cvt_pk_bf16_f32 v192, v96, v97\n\t" "v_cvt_pk_bf16_f32 v193, v98, v99\n\t" "v_cvt_pk_bf16_f32 v194, v100, v101\n\t" \
  "v_cvt_pk_bf16_f32 v195, v102, v103\n\t" "v_exp_f32 v104, v104\n\t" "v_exp_f32 v105, v105\n\t" \
  "v_mfma_f32_32x32x16_bf16 v[128:143], v[216:219], v[192:195], v[128:143]\n\t" "v_exp_f32 v106, v106\n\t" "v_exp_f32 v107, v107\n\t" "v_exp_f32 v108, v108\n\t" \
  "v_exp_f32 v109, v109\n\t" "v_exp_f32 v110, v110\n\t" "v_exp_f32 v111, v111\n\t" "v_cvt_pk_bf16_f32 v196, v104, v105\n\t" "v_cvt_pk_bf16_f32 v197, v106, v107\n\t" \
  "v_cvt_pk_bf16_f32 v198, v108, v109\n\t" "v_cvt_pk_bf16_f32 v199, v110, v111\n\t" "v_exp_f32 v112, v112\n\t" "v_exp_f32 v113, v113\n\t" \
  "v_mfma_f32_32x32x16_bf16 v[128:143], v[224:227], v[196:199], v[128:143]\n\t" "v_exp_f32 v114, v114\n\t" "v_exp_f32 v115, v115\n\t" "v_exp_f32 v116, v116\n\t" \
  "v_exp_f32 v117, v117\n\t" "v_exp_f32 v118, v118\n\t" "s_waitcnt lgkmcnt(0)\n\t" "v_mfma_f32_32x32x16_bf16 v[96:111], v[212:215], v[232:235], v[160:175]\n\t" \
  "v_exp_f32 v119, v119\n\t" "v_cvt_pk_bf16_f32 v200, v112, v113\n\t" "v_cvt_pk_bf16_f32 v201, v114, v115\n\t" "v_cvt_pk_bf16_f32 v202, v116, v117\n\t" \
  "v_cvt_pk_bf16_f32 v203, v118, v119\n\t" "v_exp_f32 v120, v120\n\t" "v_exp_f32 v121, v121\n\t" \
  "v_mfma_f32_32x32x16_bf16 v[144:159], v[216:219], v[200:203], v[144:159]\n\t" "v_exp_f32 v122, v122\n\t" "v_exp_f32 v123, v123\n\t" "v_exp_f32 v124, v124\n\t" \
  "v_exp_f32 v125, v125\n\t" "v_exp_f32 v126, v126\n\t" "v_exp_f32 v127, v127\n\t" "v_cvt_pk_bf16_f32 v204, v120, v121\n\t" "v_cvt_pk_bf16_f32 v205, v122, v123\n\t" \
  "v_cvt_pk_bf16_f32 v206, v124, v125\n\t" "v_cvt_pk_bf16_f32 v207, v126, v127\n\t" "v_exp_f32 v96, v96\n\t" "v_exp_f32 v97, v97\n\t" \
  "v_mfma_f32_32x32x16_bf16 v[144:159], v[224:227], v[204:207], v[144:159]\n\t" "ds_read_b128 v[208:211], %[ka] offset:4160\n\t" \
  "ds_read_b128 v[216:219], %[va0] offset:4160\n\t" "ds_read_b128 v[224:227], %[va1] offset:4160\n\t" "v_exp_f32 v98, v98\n\t" "v_exp_f32 v99, v99\n\t" \
  "v_exp_f32 v100, v100\n\t" "v_exp_f32 v101, v101\n\t" "v_exp_f32 v102, v102\n\t" "v_mfma_f32_32x32x16_bf16 v[112:127], v[212:215], v[236:239], v[176:191]\n\t" \
  "v_exp_f32 v103, v103\n\t" "v_cvt_pk_bf16_f32 v192, v96, v97\n\t" "v_cvt_pk_bf16_f32 v193, v98, v99\n\t" "v_cvt_pk_bf16_f32 v194, v100, v101\n\t" \
  "v_cvt_pk_bf16_f32 v195, v102, v103\n\t" "v_exp_f32 v104, v104\n\t" "v_exp_f32 v105, v105\n\t" \
  "v_mfma_f32_32x32x16_bf16 v[128:143], v[220:223], v[192:195], v[128:143]\n\t" "v_exp_f32 v106, v106\n\t" "v_exp_f32 v107, v107\n\t" "v_exp_f32 v108, v108\n\t" \
  "v_exp_f32 v109, v109\n\t" "v_exp_f32 v110, v110\n\t" "v_exp_f32 v111, v111\n\t" "v_cvt_pk_bf16_f32 v196, v104, v105\n\t" "v_cvt_pk_bf16_f32 v197, v106, v107\n\t" \
  "v_cvt_pk_bf16_f32 v198, v108, v109\n\t" "v_cvt_pk_bf16_f32 v199, v110, v111\n\t" "v_exp_f32 v112, v112\n\t" "v_exp_f32 v113, v113\n\t" \
  "v_mfma_f32_32x32x16_bf16 v[128:143], v[228:231], v[196:199], v[128:143]\n\t" "v_exp_f32 v114, v114\n\t" "v_exp_f32 v115, v115\n\t" "v_exp_f32 v116, v116\n\t" \
  "v_exp_f32 v117, v117\n\t" "v_exp_f32 v118, v118\n\t" "s_waitcnt lgkmcnt(0)\n\t" "v_mfma_f32_32x32x16_bf16 v[96:111], v[208:211], v[232:235], v[160:175]\n\t" \
  "v_exp_f32 v119, v119\n\t" "v_cvt_pk_bf16_f32 v200, v112, v113\n\t" "v_cvt_pk_bf16_f32 v201, v114, v115\n\t" "v_cvt_pk_bf16_f32 v202, v116, v117\n\t" \
  "v_cvt_pk_bf16_f32 v203, v118, v119\n\t" "v_exp_f32 v120, v120\n\t" "v_exp_f32 v121, v121\n\t" \
  "v_mfma_f32_32x32x16_bf16 v[144:159], v[220:223], v[200:203], v[144:159]\n\t" "v_exp_f32 v122, v122\n\t" "v_exp_f32 v123, v123\n\t" "v_exp_f32 v124, v124\n\t" \
  "v_exp_f32 v125, v125\n\t" "v_exp_f32 v126, v126\n\t" "v_exp_f32 v127, v127\n\t" "v_cvt_pk_bf16_f32 v204, v120, v121\n\t" "v_cvt_pk_bf16_f32 v205, v122, v123\n\t" \
  "v_cvt_pk_bf16_f32 v206, v124, v125\n\t" "v_cvt_pk_bf16_f32 v207, v126, v127\n\t" "v_exp_f32 v96, v96\n\t" "v_exp_f32 v97, v97\n\t" \
  "v_mfma_f32_32x32x16_bf16 v[144:159], v[228:231], v[204:207], v[144:159]\n\t" "ds_read_b128 v[212:215], %[ka] offset:6240\n\t" \
  "ds_read_b128 v[220:223], %[va0] offset:6240\n\t" "ds_read_b128 v[228:231], %[va1] offset:6240\n\t" "v_exp_f32 v98, v98\n\t" "v_exp_f32 v99, v99\n\t" \
  "v_exp_f32 v100, v100\n\t" "v_exp_f32 v101, v101\n\t" "v_exp_f32 v102, v102\n\t" "v_mfma_f32_32x32x16_bf16 v[112:127], v[208:211], v[236:239], v[176:191]\n\t" \
  "v_exp_f32 v103, v103\n\t" "v_cvt_pk_bf16_f32 v192, v96, v97\n\t" "v_cvt_pk_bf16_f32 v193, v98, v99\n\t" "v_cvt_pk_bf16_f32 v194, v100, v101\n\t" \
  "v_cvt_pk_bf16_f32 v195, v102, v103\n\t" "v_exp_f32 v104, v104\n\t" "v_exp_f32 v105, v105\n\t" \
  "v_mfma_f32_32x32x16_bf16 v[128:143], v[216:219], v[192:195], v[128:143]\n\t" "v_exp_f32 v106, v106\n\t" "v_exp_f32 v107, v107\n\t" "v_exp_f32 v108, v108\n\t" \
  "v_exp_f32 v109, v109\n\t" "v_exp_f32 v110, v110\n\t" "v_exp_f32 v111, v111\n\t" "v_cvt_pk_bf16_f32 v196, v104, v105\n\t" "v_cvt_pk_bf16_f32 v197, v106, v107\n\t" \
  "v_cvt_pk_bf16_f32 v198, v108, v109\n\t" "v_cvt_pk_bf16_f32 v199, v110, v111\n\t" "v_exp_f32 v112, v112\n\t" "v_exp_f32 v113, v113\n\t" \
  "v_mfma_f32_32x32x16_bf16 v[128:143], v[224:227], v[196:199], v[128:143]\n\t" "v_exp_f32 v114, v114\n\t" "v_exp_f32 v115, v115\n\t" "v_exp_f32 v116, v116\n\t" \
  "v_exp_f32 v117, v117\n\t" "v_exp_f32 v118, v118\n\t" "s_waitcnt lgkmcnt(0)\n\t" "v_mfma_f32_32x32x16_bf16 v[96:111], v[212:215], v[232:235], v[160:175]\n\t" \
  "v_exp_f32 v119, v119\n\t" "v_cvt_pk_bf16_f32 v200, v112, v113\n\t" "v_cvt_pk_bf16_f32 v201, v114, v115\n\t" "v_cvt_pk_bf16_f32 v202, v116, v117\n\t" \
  "v_cvt_pk_bf16_f32 v203, v118, v119\n\t" "v_exp_f32 v120, v120\n\t" "v_exp_f32 v121, v121\n\t" \
  "v_mfma_f32_32x32x16_bf16 v[144:159], v[216:219], v[200:203], v[144:159]\n\t" "v_exp_f32 v122, v122\n\t" "v_exp_f32 v123, v123\n\t" "v_exp_f32 v124, v124\n\t" \
  "v_exp_f32 v125, v125\n\t" "v_exp_f32 v126, v126\n\t" "v_exp_f32 v127, v127\n\t" "v_cvt_pk_bf16_f32 v204, v120, v121\n\t" "v_cvt_pk_bf16_f32 v205, v122, v123\n\t" \
  "v_cvt_pk_bf16_f32 v206, v124, v125\n\t" "v_cvt_pk_bf16_f32 v207, v126, v127\n\t" "v_exp_f32 v96, v96\n\t" "v_exp_f32 v97, v97\n\t" \
  "v_mfma_f32_32x32x16_bf16 v[144:159], v[224:227], v[204:207], v[144:159]\n\t" "ds_read_b128 v[208:211], %[ka] offset:8320\n\t" \
  "ds_read_b128 v[216:219], %[va0] offset:8320\n\t" "ds_read_b128 v[224:227], %[va1] offset:8320\n\t" "v_exp_f32 v98, v98\n\t" "v_exp_f32 v99, v99\n\t" \
  "v_exp_f32 v100, v100\n\t" "v_exp_f32 v101, v101\n\t" "v_exp_f32 v102, v102\n\t" "v_mfma_f32_32x32x16_bf16 v[112:127], v[212:215], v[236:239], v[176:191]\n\t" \
  "v_exp_f32 v103, v103\n\t" "v_cvt_pk_bf16_f32 v192, v96, v97\n\t" "v_cvt_pk_bf16_f32 v193, v98, v99\n\t" "v_cvt_pk_bf16_f32 v194, v100, v101\n\t" \
  "v_cvt_pk_bf16_f32 v195, v102, v103\n\t" "v_exp_f32 v104, v104\n\t" "v_exp_f32 v105, v105\n\t" \
  "v_mfma_f32_32x32x16_bf16 v[128:143], v[220:223], v[192:195], v[128:143]\n\t" "v_exp_f32 v106, v106\n\t" "v_exp_f32 v107, v107\n\t" "v_exp_f32 v108, v108\n\t" \
  "v_exp_f32 v109, v109\n\t" "v_exp_f32 v110, v110\n\t" "v_exp_f32 v111, v111\n\t" "v_cvt_pk_bf16_f32 v196, v104, v105\n\t" "v_cvt_pk_bf16_f32 v197, v106, v107\n\t" \
  "v_cvt_pk_bf16_f32 v198, v108, v109\n\t" "v_cvt_pk_bf16_f32 v199, v110, v111\n\t" "v_exp_f32 v112, v112\n\t" "v_exp_f32 v113, v113\n\t" \
  "v_mfma_f32_32x32x16_bf16 v[128:143], v[228:231], v[196:199], v[128:143]\n\t" "v_exp_f32 v114, v114\n\t" "v_exp_f32 v115, v115\n\t" "v_exp_f32 v116, v116\n\t" \
  "v_exp_f32 v117, v117\n\t" "v_exp_f32 v118, v118\n\t" "s_waitcnt lgkmcnt(0)\n\t" "v_mfma_f32_32x32x16_bf16 v[96:111], v[208:211], v[232:235], v[160:175]\n\t" \
  "v_exp_f32 v119, v119\n\t" "v_cvt_pk_bf16_f32 v200, v112, v113\n\t" "v_cvt_pk_bf16_f32 v201, v114, v115\n\t" "v_cvt_pk_bf16_f32 v202, v116, v117\n\t" \
  "v_cvt_pk_bf16_f32 v203, v118, v119\n\t" "v_exp_f32 v120, v120\n\t" "v_exp_f32 v121, v121\n\t" \
  "v_mfma_f32_32x32x16_bf16 v[144:159], v[220:223], v[200:203], v[144:159]\n\t" "v_exp_f32 v122, v122\n\t" "v_exp_f32 v123, v123\n\t" "v_exp_f32 v124, v124\n\t" \
  "v_exp_f32 v125, v125\n\t" "v_exp_f32 v126, v126\n\t" "v_exp_f32 v127, v127\n\t" "v_cvt_pk_bf16_f32 v204, v120, v121\n\t" "v_cvt_pk_bf16_f32 v205, v122, v123\n\t" \
  "v_cvt_pk_bf16_f32 v206, v124, v125\n\t" "v_cvt_pk_bf16_f32 v207, v126, v127\n\t" "v_add_u32 %[ka], 8320, %[ka]\n\t" "v_add_u32 %[va0], 8320, %[va0]\n\t" \
  "v_add_u32 %[va1], 8320, %[va1]\n\t" "s_sub_u32 %[cnt], %[cnt], 1\n\t" "s_cmp_lg_u32 %[cnt], %[ev]\n\t" "s_cbranch_scc1 1b\n\t" "s_cmp_eq_u32 %[cnt], 0\n\t" \
  "s_cbranch_scc1 4f\n\t" "s_cmp_eq_u32 %[cnt], 8\n\t" "s_cbranch_scc1 3f\n\t" "2:\n\t" "ds_read_b32 v240, %[ctr] offset:%c[o1]\n\t" "s_waitcnt lgkmcnt(0)\n\t" \
  "v_readfirstlane_b32 %[ev], v240\n\t" "s_nop 1\n\t" "s_cmp_lt_u32 %[ev], 4\n\t" "s_cbranch_scc1 2b\n\t" "s_mov_b32 %[ev], 8\n\t" "s_branch 1b\n\t" "3:\n\t" \
  "s_mov_b64 exec, 1\n\t" "ds_add_u32 %[ctr], v241 offset:%c[o2]\n\t" "s_mov_b64 exec, -1\n\t" "s_mov_b32 %[ev], 0\n\t" "s_branch 1b\n\t" "4:\n\t" \
  "v_mfma_f32_32x32x16_bf16 v[144:159], v[228:231], v[204:207], v[144:159]\n\t" "s_waitcnt lgkmcnt(0)\n\t" "s_nop 7\n\t" "s_nop 3\n\t"
#define ATTN_OA "=&{v[128:143]}"
#define ATTN_OB "=&{v[144:159]}"
#define ATTN_QA "{v[232:235]}"
#define ATTN_QB "{v[236:239]}"
#define ATTN_CLOBBERS \
  "v96", "v97", "v98", "v99", "v100", "v101", "v102", "v103", "v104", "v105", "v106", "v107", "v108", "v109", "v110", "v111", "v112", "v113", "v114", "v115", \
  "v116", "v117", "v118", "v119", "v120", "v121", "v122", "v123", "v124", "v125", "v126", "v127", "v160", "v161", "v162", "v163", "v164", "v165", "v166", "v167", \
  "v168", "v169", "v170", "v171", "v172", "v173", "v174", "v175", "v176", "v177", "v178", "v179", "v180", "v181", "v182", "v183", "v184", "v185", "v186", "v187", \
  "v188", "v189", "v190", "v191", "v192", "v193", "v194", "v195", "v196", "v197", "v198", "v199", "v200", "v201", "v202", "v203", "v204", "v205", "v206", "v207", \
  "v208", "v209", "v210", "v211", "v212", "v213", "v214", "v215", "v216", "v217", "v218", "v219", "v220", "v221", "v222", "v223", "v224", "v225", "v226", "v227", \
  "v228", "v229", "v230", "v231", "v240", "v241"

DI void row_max(const char* lds, bf16x8 qa, bf16x8 qb, int lane, float& ma, float& mb) {
  f32x16 z;
#pragma unroll
  for (int i = 0; i < 16; ++i) z[i] = 0.f;
  ma = mb = -INFINITY;
#pragma unroll 1
  for (int t = 0; t < 64; ++t) {
    const bf16x8 kf = *(const bf16x8*)(lds + t * KVREC + lane * 16);
    const f32x16 sa = __builtin_amdgcn_mfma_f32_32x32x16_bf16(kf, qa, z, 0, 0, 0), sb = __builtin_amdgcn_mfma_f32_32x32x16_bf16(kf, qb, z, 0, 0, 0);
#pragma unroll
    for (int i = 0; i < 16; ++i) { ma = fmaxf(ma, sa[i]); mb = fmaxf(mb, sb[i]); }
  }
  ma = fmaxf(ma, __shfl_xor(ma, 32)); mb = fmaxf(mb, __shfl_xor(mb, 32));
}
DI float frag_norm2(u32x4 q) {
  float n = 0.f;
#pragma unroll
  for (int i = 0; i < 4; ++i) { const float a = bf_lo(q[i]), b = bf_hi(q[i]); n += a * a + b * b; }
  const unsigned w = __float_as_uint(n);
  const auto b = __builtin_amdgcn_permlane32_swap(w, w, false, false);
  return __uint_as_float(b[0]) + __uint_as_float(b[1]);
}

DI void o_chunk(char* ws, rsrc_t rs, char* lds, unsigned lb, int tb, int kc, int hf, unsigned tag, int lane) {
  wait_tags<2>(rs, WS_FO + tb * FSTR + kc * 256 + (lane & 1) * 128, tag);
  ctr_wait(lds, C_LOOP, 4);
#pragma unroll
  for (int i = 0; i < 8; ++i) stage_x_piece<true>(ws + WS_O, tb, kc, 8 * hf + i, lb + L_XC, lane);
  WAIT_VM(0);
  ctr_arrive(lds, C_XC0 + kc, lane);
}
__global__ __launch_bounds__(512, 2) void fused_kernel(const u16* __restrict__ x, const u16* __restrict__ wq, const u16* __restrict__ wk,
                                                       const u16* __restrict__ wv, const u16* __restrict__ wo, const u16* __restrict__ wr,
                                                       const u16* __restrict__ wi, const u16* __restrict__ gw, char* ws, u16* __restrict__ out) {
  __shared__ __attribute__((aligned(16))) char lds[LDS_BYTES];
  int lane = threadIdx.x & 63;
  const int wave = __builtin_amdgcn_readfirstlane(threadIdx.x >> 6);
  const int bid = blockIdx.x, tb = (bid & 7) + 8 * ((bid >> 3) & 3), cb = bid >> 5;
  const unsigned lb = (unsigned)(size_t)(LAS char*)lds;
  const rsrc_t rs = __builtin_amdgcn_make_buffer_rsrc(ws, 0, WS_END, 0x00020000);
  if (wave < 4) {
    const int th = wave & 1, hp = wave >> 1;
    __builtin_amdgcn_s_setprio(3);
    unsigned tag;
    unsigned ep = 0;
    if (wave == 3) ep = __hip_atomic_load((GAS unsigned*)(ws + WS_EPOCH), __ATOMIC_RELAXED, __HIP_MEMORY_SCOPE_AGENT);
    kv_piece(x, (cb & 1) ? wv : wk, rs, lds, lb, tb, cb, wave, lane);
    if (wave == 3) {
      tag = ep + 1;
      if (lane == 0) *(unsigned*)(lds + L_TAG) = tag;
      WAIT_LGKM0();
      ctr_arrive(lds, C_WKV, lane);
    }
    f32x16 acc;
#pragma unroll
    for (int i = 0; i < 16; ++i) acc[i] = 0.f;
    if (wave == 3) publish(ws, set_tag_off(0, bid), tag, lane);
#pragma unroll 1
    for (int kc = 0; kc < 4; ++kc) { ctr_wait(lds, C_XA0 + kc, 4); gemm_chunk(lds + L_XA, lds + L_WA, kc, th, hp, lane, acc); }
    WAIT_LGKM0();
    ctr_arrive(lds, C_GEMM, lane);
    const float QS = 0.25f * 1.44269504088896340736f;
    u32x4 qa, qb;
#pragma unroll
    for (int i = 0; i < 4; ++i) { qa[i] = pk_bf16(acc[2 * i] * QS, acc[2 * i + 1] * QS); qb[i] = pk_bf16(acc[8 + 2 * i] * QS, acc[9 + 2 * i] * QS); }
    const float na = frag_norm2(qa), nb = frag_norm2(qb);
    ctr_wait(lds, C_WKV, 1);
    tag = *(const unsigned*)(lds + L_TAG);
    if (wave == 0) {
      wait_tags<2>(rs, set_lane_off(0, lane), tag);
      ctr_arrive(lds, C_KVOK, lane);
      if (bid == 0 && lane == 0) __hip_atomic_store((GAS unsigned*)(ws + WS_EPOCH), tag, __ATOMIC_RELAXED, __HIP_MEMORY_SCOPE_AGENT);
    }
    ctr_wait(lds, C_KVOK, 1);
    float kn, kn1;
    asm volatile("global_load_dword %0, %2, off sc1\n\tglobal_load_dword %1, %2, off offset:256 sc1" : "=&v"(kn), "=&v"(kn1) : "v"(ws + WS_KN + lane * 4) : "memory");
    ctr_wait(lds, C_GEMM, 4);
#pragma unroll 1
    for (int i = 0; i < 8; ++i) kv_dma(ws, lb, 4 * i + wave, lane);
    if (lane < 32) *(u32x4*)(lds + (16 * wave + (lane >> 1)) * KVREC + 2048 + (lane & 1) * 16) = (lane & 1) ? u32x4{0, 0, 0, 0} : u32x4{0x3f803f80u, 0x3f803f80u, 0x3f803f80u, 0x3f803f80u};
    ctr_arrive(lds, C_KVM, lane);
    asm volatile("s_waitcnt vmcnt(8)" : "+v"(kn), "+v"(kn1) :: "memory");
    kn = __uint_as_float(wave_umax(max(__float_as_uint(kn), __float_as_uint(kn1))));
    float ba = sqrtf(na * kn) * 1.001f + 1e-6f, bb = sqrtf(nb * kn) * 1.001f + 1e-6f;
    unsigned inc = 1;
    WAIT_LGKM0();
    if (!__all(ba <= 48.f && bb <= 48.f)) {
      WAIT_VM(0);
      ctr_arrive(lds, C_KV0, lane); inc = 0;
      ctr_wait(lds, C_KV0, 4);
      ctr_wait(lds, C_KV1, 4);
      row_max(lds, __builtin_bit_cast(bf16x8, qa), __builtin_bit_cast(bf16x8, qb), lane, ba, bb);
    }
    f32x16 oa, ob;
    {
      const int r = lane & 31, h2 = lane >> 5;
      const bool cst = r >= 16;
      unsigned ka = lb + lane * 16;
      unsigned va0 = lb + (cst ? ((r == 16 || r == 20) ? 2048 : 2064) : 1024 + h2 * 256 + r * 16);
      unsigned va1 = cst ? va0 : va0 + 512;
      unsigned cnt, ev;
      const float ca = -ba, cbv = -bb;
      asm volatile(ATTN_ASM
        : [ka] "+v"(ka), [va0] "+v"(va0), [va1] "+v"(va1), [cnt] "=&s"(cnt), [ev] "=&s"(ev), ATTN_OA(oa), ATTN_OB(ob)
        : [ca] "v"(ca), [cb] "v"(cbv), ATTN_QA(qa), ATTN_QB(qb), [ctr] "v"(lb + L_CTR), [inc] "v"(inc), [o0] "i"(C_KV0 * 4), [o1] "i"(C_KV1 * 4), [o2] "i"(C_HALF * 4)
        : "memory", "scc", ATTN_CLOBBERS);
    }
    __builtin_amdgcn_s_setprio(3);
    asm volatile("" : "+v"(lane));
    {
      const float ia = __builtin_amdgcn_rcpf(oa[8]), ib = __builtin_amdgcn_rcpf(ob[8]);
      u32x4 va, vb;
#pragma unroll
      for (int i = 0; i < 4; ++i) { va[i] = pk_bf16(oa[2 * i] * ia, oa[2 * i + 1] * ia); vb[i] = pk_bf16(ob[2 * i] * ib, ob[2 * i + 1] * ib); }
      const int e = (lane >> 1) & 1;
      u32x4 s0, s1;
#pragma unroll
      for (int i = 0; i < 4; ++i) {
        const int src0 = ((lane >> 2) + 32 * (lane & 1)) * 4;
        const unsigned a0 = __builtin_amdgcn_ds_bpermute(src0, va[i]), b0 = __builtin_amdgcn_ds_bpermute(src0, vb[i]);
        const unsigned a1 = __builtin_amdgcn_ds_bpermute(src0 + 64, va[i]), b1 = __builtin_amdgcn_ds_bpermute(src0 + 64, vb[i]);
        s0[i] = e ? b0 : a0; s1[i] = e ? b1 : a1;
      }
      const unsigned off = WS_O + (64 * tb + 32 * th + (lane >> 2)) * 1024 + (4 * cb + 2 * hp) * 32 + (lane & 3) * 16;
      st16(rs, off, s0); st16(rs, off + 16 * 1024, s1);
    }
    ctr_arrive(lds, C_LOOP, lane);
    bf16x8 wfr[32];
    publish4(lds, C_ODR, rs, WS_FO + tb * FSTR + cb * 128, tag, lane);
    ctr_wait(lds, C_WO, 4);
    gemm_load_w(lds + L_WC, hp, lane, wfr);
    o_chunk(ws, rs, lds, lb, tb, wave, 1, tag, lane);
    const int tok = lane & 31, h2 = lane >> 5, t = 32 * th + tok;
#pragma unroll
    for (int i = 0; i < 16; ++i) acc[i] = 0.f;
    ctr_wait(lds, C_YT, 4);
    const float gg = *(const float*)(lds + L_FS + FSCR + t * 4);
    u32x4 yq[2];
#pragma unroll
    for (int e = 0; e < 2; ++e) yq[e] = *(const u32x4*)(lds + L_YT + t * 128 + (((4 * hp + 2 * e + h2) ^ (t & 7)) << 4));
    bf16x8 xb0[8], xb1[8];
    ctr_wait(lds, C_XC0, 2); gemm_load_x(lds + L_XC, 0, th, lane, xb0);
    ctr_wait(lds, C_XC1, 2); gemm_load_x(lds + L_XC, 1, th, lane, xb1); gemm_mm<0>(wfr, xb0, acc);
    ctr_wait(lds, C_XC2, 2); gemm_load_x(lds + L_XC, 2, th, lane, xb0); gemm_mm<1>(wfr, xb1, acc);
    ctr_wait(lds, C_XC3, 2); gemm_load_x(lds + L_XC, 3, th, lane, xb1); gemm_mm<2>(wfr, xb0, acc);
    gemm_mm<3>(wfr, xb1, acc);
    const int n = 8 * tb + (t >> 3) + 256 * (t & 7);
#pragma unroll
    for (int e = 0; e < 2; ++e) {
      const u32x4 yv = yq[e];
      u32x4 ov;
#pragma unroll
      for (int i = 0; i < 4; ++i)
        ov[i] = pk_bf16(fmaf(gg, acc[8 * e + 2 * i], bf_lo(yv[i])), fmaf(gg, acc[8 * e + 2 * i + 1], bf_hi(yv[i])));
      *(u32x4*)(out + (size_t)n * 512 + 64 * cb + 32 * hp + 16 * e + 8 * h2) = ov;
    }
  } else {
    __builtin_amdgcn_s_setprio(2);
    const int p = wave - 4, n0 = 8 * tb + cb;
    char* scr = lds + L_FS + p * FSCR;
    Tw14 t5, t2;
    if (wave == 4 && lane < 32) ((LAS unsigned*)(lds + L_CTR))[lane] = 0;
    WAIT_LGKM0();
    __builtin_amdgcn_s_barrier();
#define TILE_DMA(kc) _Pragma("unroll") for (int i = 0; i < 4; ++i) { \
      stage_x_piece<false>((const char*)x, tb, kc, 4 * p + i, lb + L_XA, lane); stage_w_piece(wq, cb, 16 * (kc) + 4 * p + i, lb + L_WA, lane); }
    TILE_DMA(0) TILE_DMA(1) TILE_DMA(2)
    WAIT_VM(16); ctr_arrive(lds, C_XA0, lane);
    TILE_DMA(3)
    WAIT_VM(16); ctr_arrive(lds, C_XA1, lane);
    WAIT_VM(8);  ctr_arrive(lds, C_XA2, lane);
    WAIT_VM(0);  ctr_arrive(lds, C_XA3, lane);
    u16 wsv[8];
    load_tw(t5, 0, lane);
    wspec_load(wr, wi, bid, p, lane, wsv);
    const int cq = (bid & 7) * 8 + ((bid >> 3) & 7), pf = bid >> 6;
    int kvs = 0;
    f1_stage(x, gw, ws, lb, kvs, t2, rs, lds, n0, p, t5, scr, lane);
    wspec_store(wsv, rs, bid, p, lane);
    WAIT_LGKM0();
    ctr_arrive(lds, C_H0, lane);
    kv_rest(ws, lds, lb, p, lane, true, kvs);
    const unsigned tag = *(const unsigned*)(lds + L_TAG);
    publish(ws, set_tag_off(1 + p, bid), tag, lane);
    if (kvs == 1) ctr_arrive(lds, C_KV1, lane);
    U4H w_re[3], w_im[3];
    if (p != 0) w_load(wr, wi, cq, pf, p, lane, w_re, w_im);
    if (p == 0) { wait_set<4>(ws, rs, 1 + pf, (bid & 63) == 9 * pf, bid & 7, tag, lane); ctr_arrive(lds, C_A1OK, lane); }
    else ctr_wait<4>(lds, C_A1OK, 1);
    f2_stage(w_re, w_im, rs, lds, cq, pf, p, t2, lane);
    publish(ws, set_tag_off(5 + pf, cq * 4 + p), tag, lane);
    bool wo_req = false;
    if (bid != 36 + 9 * p && __builtin_amdgcn_readfirstlane(__hip_atomic_load((LAS unsigned*)(lds + L_CTR) + C_HALF, __ATOMIC_RELAXED, __HIP_MEMORY_SCOPE_WORKGROUP)) >= 4) {
#pragma unroll 1
      for (int i = 0; i < 16; ++i) stage_w_piece(wo, cb, 16 * p + i, lb + L_WC, lane);
      wo_req = true;
    }
    wait_set<4>(ws, rs, 5 + p, bid == 36 + 9 * p, bid & 7, tag, lane);
    if (wo_req) { WAIT_VM(0); ctr_arrive(lds, C_WO, lane); }
    ctr_wait(lds, C_H5, 4);
    f3_stage(rs, n0, p, t5, scr, lane);
    publish4(lds, C_TDR, rs, WS_FT1 + tb * FSTR + cb * 128, tag, lane);
    if (!wo_req) {
      ctr_wait<16>(lds, C_HALF, 4);
#pragma unroll 1
      for (int i = 0; i < 16; ++i) stage_w_piece(wo, cb, 16 * p + i, lb + L_WC, lane);
      WAIT_VM(0);
      ctr_arrive(lds, C_WO, lane);
    }
    if (p == 0) { wait_tags<6>(rs, WS_FT1 + tb * FSTR + (lane & 7) * 128, tag); ctr_arrive(lds, C_T1OK, lane); }
    else ctr_wait<4>(lds, C_T1OK, 1);
    YIn yin;
    y_load(rs, tb, cb, p, lane, yin);
    if (p == 1) { *(unsigned*)(lds + L_FS + FSCR + lane * 4) = ld4(rs, WS_G + (64 * tb + lane) * 4); WAIT_LGKM0(); }
    ctr_wait<8>(lds, C_LOOP, 1);
    y_stage(yin, lds, p, lane);
    ctr_arrive(lds, C_YT, lane);
    o_chunk(ws, rs, lds, lb, tb, p, 0, tag, lane);
  }
}

extern "C" void kernel_launch(void* const* d_in, const int* in_sizes, int n_in, void* d_out, int out_size, void* d_ws, size_t ws_size,
                              hipStream_t stream) {
  fused_kernel<<<256, 512, 0, stream>>>((const u16*)d_in[0], (const u16*)d_in[1], (const u16*)d_in[2], (const u16*)d_in[3], (const u16*)d_in[4],
                                        (const u16*)d_in[6], (const u16*)d_in[7], (const u16*)d_in[8], (char*)d_ws, (u16*)d_out);
}
```
